```python
import torch

def _softmax(x, axis=-1):
    return torch.softmax(x, dim=axis)
def _gelu(x):
    return torch.nn.functional.gelu(x, approximate="tanh")
def _silu(x):
    return torch.nn.functional.silu(x)
def _sigmoid(x):
    return torch.sigmoid(x)
def _layernorm(x, axis=-1, eps=1e-5):
    m = x.mean(dim=axis, keepdim=True)
    v = x.var(dim=axis, keepdim=True, unbiased=False)
    return (x - m) / torch.sqrt(v + eps)
def _scatter_add(tgt, idx, src, axis=0):
    return tgt.clone().index_add_(axis, idx, src)
def _segment_sum(src, seg, n):
    out = torch.zeros((n,) + src.shape[1:], dtype=src.dtype)
    return out.index_add_(0, seg, src)
def _pad(x, pw):
    flat = tuple(v for pair in reversed(pw) for v in pair)
    return torch.nn.functional.pad(x, flat)

kernel_name = 'standalone__rezero__sampling'

def setup_inputs():

    _w = (lambda a: (lambda t: t.to(torch.bfloat16) if t.is_floating_point() else t)(torch.as_tensor(a)))
    B, S, D = 1024, 8, 640
    return {
        "x": _w((lambda *s: torch.randn(*s, dtype=torch.bfloat16) * 0.05)(B, S, D)),
        "fx": _w((lambda *s: torch.randn(*s, dtype=torch.bfloat16) * 0.8)(B, S, D)),
        "alpha": _w((lambda *s: torch.randn(*s, dtype=torch.bfloat16) * 0.8)(1) * 0.1),
    }

def reference(x, fx, alpha):
    return x + alpha * fx
```

```cpp
#include <hip/hip_runtime.h>
#include <hip/hip_bf16.h>
#include <stdint.h>

typedef __attribute__((ext_vector_type(4))) unsigned int u32x4;
typedef __attribute__((ext_vector_type(2))) float f32x2;
typedef __attribute__((ext_vector_type(2))) __bf16 bf16x2;

#define BLOCK 256
#define VPT 2

__device__ __forceinline__ unsigned rz_pair(unsigned xw, unsigned fw, float a) {
    float x0 = __uint_as_float(xw << 16), x1 = __uint_as_float(xw & 0xffff0000u);
    float f0 = __uint_as_float(fw << 16), f1 = __uint_as_float(fw & 0xffff0000u);
    f32x2 p = {a * f0, a * f1};
    bf16x2 pb = __builtin_convertvector(p, bf16x2);
    unsigned pw = __builtin_bit_cast(unsigned, pb);
    float p0 = __uint_as_float(pw << 16), p1 = __uint_as_float(pw & 0xffff0000u);
    f32x2 s = {x0 + p0, x1 + p1};
    bf16x2 sb = __builtin_convertvector(s, bf16x2);
    return __builtin_bit_cast(unsigned, sb);
}

__global__ __launch_bounds__(BLOCK) void rezero_kernel(const u32x4* __restrict__ x,
                                                       const u32x4* __restrict__ fx,
                                                       const unsigned short* __restrict__ alpha,
                                                       u32x4* __restrict__ out, int nvec) {
    float a = __uint_as_float(((unsigned)alpha[0]) << 16);
    int base = blockIdx.x * (BLOCK * VPT) + threadIdx.x;
    u32x4 xv[VPT], fv[VPT];
#pragma unroll
    for (int k = 0; k < VPT; ++k) {
        int i = base + k * BLOCK;
        if (i < nvec) { xv[k] = x[i]; fv[k] = fx[i]; }
    }
#pragma unroll
    for (int k = 0; k < VPT; ++k) {
        int i = base + k * BLOCK;
        if (i < nvec) {
            u32x4 o;
#pragma unroll
            for (int j = 0; j < 4; ++j) o[j] = rz_pair(xv[k][j], fv[k][j], a);
            out[i] = o;
        }
    }
}

__global__ void rezero_tail(const unsigned short* __restrict__ x, const unsigned short* __restrict__ fx,
                            const unsigned short* __restrict__ alpha, unsigned short* __restrict__ out,
                            int start, int n) {
    int i = start + threadIdx.x;
    if (i < n) {
        float a = __uint_as_float(((unsigned)alpha[0]) << 16);
        unsigned r = rz_pair(x[i], fx[i], a);
        out[i] = (unsigned short)(r & 0xffffu);
    }
}

extern "C" void kernel_launch(void* const* d_in, const int* in_sizes, int n_in,
                              void* d_out, int out_size, void* d_ws, size_t ws_size,
                              hipStream_t stream) {
    const int n = out_size;
    const int nvec = n / 8;
    const int grid = (nvec + BLOCK * VPT - 1) / (BLOCK * VPT);
    if (grid > 0)
        rezero_kernel<<<grid, BLOCK, 0, stream>>>((const u32x4*)d_in[0], (const u32x4*)d_in[1],
                                                  (const unsigned short*)d_in[2], (u32x4*)d_out, nvec);
    if (n % 8)
        rezero_tail<<<1, 64, 0, stream>>>((const unsigned short*)d_in[0], (const unsigned short*)d_in[1],
                                          (const unsigned short*)d_in[2], (unsigned short*)d_out, nvec * 8, n);
}
```

```cpp
#include <hip/hip_runtime.h>
#include <stdint.h>

#define RZ_BLOCK 64
#define RZ_TILE_ELEMS (RZ_BLOCK * 8)

typedef __attribute__((ext_vector_type(4))) unsigned int u32x4;
typedef __attribute__((ext_vector_type(2))) float f32x2;
typedef __attribute__((ext_vector_type(2))) __bf16 bf16x2;

__device__ __forceinline__ float rz_round(float v) {
    const f32x2 p = {0.f, v};
    return __uint_as_float(__builtin_bit_cast(unsigned, __builtin_convertvector(p, bf16x2)));
}

__device__ __forceinline__ unsigned rz_pair(unsigned xw, unsigned fw, float a) {
    const f32x2 f = {__uint_as_float(fw << 16), __uint_as_float(fw & 0xffff0000u)};
    const f32x2 av = {a, a};
    const f32x2 p = f * av;
    const f32x2 pr = {rz_round(p[0]), rz_round(p[1])};
    const f32x2 xf = {__uint_as_float(xw << 16), __uint_as_float(xw & 0xffff0000u)};
    const f32x2 s = xf + pr;
    return __builtin_bit_cast(unsigned, __builtin_convertvector(s, bf16x2));
}

__global__ __launch_bounds__(RZ_BLOCK) void rezero_main(const void* __restrict__ x,
                                                        const void* __restrict__ fx,
                                                        const unsigned* __restrict__ alpha,
                                                        void* __restrict__ out) {
    const unsigned off = (blockIdx.x * RZ_BLOCK + threadIdx.x) * 16u;
    const u32x4 fv = __builtin_nontemporal_load((const u32x4*)((const char*)fx + off));
    const u32x4 xv = __builtin_nontemporal_load((const u32x4*)((const char*)x + off));
    const float a = __uint_as_float(alpha[0] << 16);
    u32x4 o;
#pragma unroll
    for (int j = 0; j < 4; ++j) o[j] = rz_pair(xv[j], fv[j], a);
    *(u32x4*)((char*)out + off) = o;
}

__global__ __launch_bounds__(256) void rezero_generic(const unsigned short* __restrict__ x,
                                                      const unsigned short* __restrict__ fx,
                                                      const unsigned short* __restrict__ alpha,
                                                      unsigned short* __restrict__ out,
                                                      long long start, long long n) {
    const long long i = start + (long long)blockIdx.x * 256 + threadIdx.x;
    if (i < n) {
        const float a = __uint_as_float((unsigned)alpha[0] << 16);
        out[i] = (unsigned short)(rz_pair(x[i], fx[i], a) & 0xffffu);
    }
}

extern "C" void kernel_launch(void* const* d_in, const int* in_sizes, int n_in, void* d_out,
                              int out_size, void* d_ws, size_t ws_size, hipStream_t stream) {
    (void)in_sizes; (void)n_in; (void)d_ws; (void)ws_size;
    const long long n = out_size;
    const uintptr_t al = (uintptr_t)d_in[0] | (uintptr_t)d_in[1] | (uintptr_t)d_out;
    long long tiles = n / RZ_TILE_ELEMS;
    if ((al & 15) || ((uintptr_t)d_in[2] & 3)) tiles = 0;
    if (tiles > 0)
        rezero_main<<<dim3((unsigned)tiles), dim3(RZ_BLOCK), 0, stream>>>(
            d_in[0], d_in[1], (const unsigned*)d_in[2], d_out);
    const long long done = tiles * RZ_TILE_ELEMS;
    if (done < n)
        rezero_generic<<<dim3((unsigned)((n - done + 255) / 256)), dim3(256), 0, stream>>>(
            (const unsigned short*)d_in[0], (const unsigned short*)d_in[1],
            (const unsigned short*)d_in[2], (unsigned short*)d_out, done, n);
}
```

```cpp
#define RZ_P 11

#include <hip/hip_runtime.h>
#include <stdint.h>

#define RZ_BLOCK 64
#define RZ_CHUNK_ELEMS (RZ_BLOCK * 8)
#define RZ_TILE_ELEMS ((RZ_P - 1) * 8 * RZ_CHUNK_ELEMS)

typedef __attribute__((ext_vector_type(4))) unsigned int u32x4;
typedef __attribute__((ext_vector_type(2))) float f32x2;
typedef __attribute__((ext_vector_type(2))) __bf16 bf16x2;

__device__ __forceinline__ float rz_round(float v) {
    const f32x2 p = {0.f, v};
    return __uint_as_float(__builtin_bit_cast(unsigned, __builtin_convertvector(p, bf16x2)));
}

__device__ __forceinline__ unsigned rz_pair(unsigned xw, unsigned fw, float a) {
    const f32x2 f = {__uint_as_float(fw << 16), __uint_as_float(fw & 0xffff0000u)};
    const f32x2 av = {a, a};
    const f32x2 p = f * av;
    const f32x2 pr = {rz_round(p[0]), rz_round(p[1])};
    const f32x2 xf = {__uint_as_float(xw << 16), __uint_as_float(xw & 0xffff0000u)};
    const f32x2 s = xf + pr;
    return __builtin_bit_cast(unsigned, __builtin_convertvector(s, bf16x2));
}

__global__ __launch_bounds__(RZ_BLOCK) void rezero_main(const void* __restrict__ x,
                                                        const void* __restrict__ fx,
                                                        const unsigned* __restrict__ alpha,
                                                        void* __restrict__ out) {
    const unsigned g = blockIdx.x >> 3, q = g / RZ_P, m = g - q * RZ_P;
    const unsigned chunk = (q * (RZ_P - 1) + m) * 8u + (blockIdx.x & 7u);
    const unsigned off = (chunk * RZ_BLOCK + threadIdx.x) * 16u;
    asm volatile("" ::"s"(x), "s"(fx), "s"(alpha), "s"(out), "v"(off));
    if (m == RZ_P - 1) return;
    const u32x4 fv = __builtin_nontemporal_load((const u32x4*)((const char*)fx + off));
    const u32x4 xv = __builtin_nontemporal_load((const u32x4*)((const char*)x + off));
    typedef const __attribute__((address_space(4))) unsigned* cptr;
    const float a = __uint_as_float(*(cptr)(uintptr_t)alpha << 16);
    u32x4 o;
#pragma unroll
    for (int j = 0; j < 4; ++j) o[j] = rz_pair(xv[j], fv[j], a);
    *(u32x4*)((char*)out + off) = o;
}

__global__ __launch_bounds__(256) void rezero_generic(const unsigned short* __restrict__ x,
                                                      const unsigned short* __restrict__ fx,
                                                      const unsigned short* __restrict__ alpha,
                                                      unsigned short* __restrict__ out,
                                                      long long start, long long n) {
    const long long i = start + (long long)blockIdx.x * 256 + threadIdx.x;
    if (i < n) {
        const float a = __uint_as_float((unsigned)alpha[0] << 16);
        out[i] = (unsigned short)(rz_pair(x[i], fx[i], a) & 0xffffu);
    }
}

extern "C" void kernel_launch(void* const* d_in, const int* in_sizes, int n_in, void* d_out,
                              int out_size, void* d_ws, size_t ws_size, hipStream_t stream) {
    (void)in_sizes; (void)n_in; (void)d_ws; (void)ws_size;
    const long long n = out_size;
    const uintptr_t al = (uintptr_t)d_in[0] | (uintptr_t)d_in[1] | (uintptr_t)d_out;
    long long tiles = n / RZ_TILE_ELEMS;
    if ((al & 15) || ((uintptr_t)d_in[2] & 3)) tiles = 0;
    if (tiles > 0)
        rezero_main<<<dim3((unsigned)tiles * (RZ_P * 8)), dim3(RZ_BLOCK), 0, stream>>>(
            d_in[0], d_in[1], (const unsigned*)d_in[2], d_out);
    const long long done = tiles * RZ_TILE_ELEMS;
    if (done < n)
        rezero_generic<<<dim3((unsigned)((n - done + 255) / 256)), dim3(256), 0, stream>>>(
            (const unsigned short*)d_in[0], (const unsigned short*)d_in[1],
            (const unsigned short*)d_in[2], (unsigned short*)d_out, done, n);
}
```

```cpp
#include <hip/hip_runtime.h>
#include <stdint.h>

#define RZ_BLOCK 64
#define RZ_TILE_ELEMS (RZ_BLOCK * 8)

typedef __attribute__((ext_vector_type(4))) unsigned int u32x4;
typedef __attribute__((ext_vector_type(2))) float f32x2;
typedef __attribute__((ext_vector_type(2))) __bf16 bf16x2;

__device__ __forceinline__ float rz_round(float v) {
    const f32x2 p = {0.f, v};
    return __uint_as_float(__builtin_bit_cast(unsigned, __builtin_convertvector(p, bf16x2)));
}

__device__ __forceinline__ unsigned rz_pair(unsigned xw, unsigned fw, float a) {
    const f32x2 f = {__uint_as_float(fw << 16), __uint_as_float(fw & 0xffff0000u)};
    const f32x2 av = {a, a};
    const f32x2 p = f * av;
    const f32x2 pr = {rz_round(p[0]), rz_round(p[1])};
    const f32x2 xf = {__uint_as_float(xw << 16), __uint_as_float(xw & 0xffff0000u)};
    const f32x2 s = xf + pr;
    return __builtin_bit_cast(unsigned, __builtin_convertvector(s, bf16x2));
}

__global__ __launch_bounds__(RZ_BLOCK) void rezero_main(const void* __restrict__ x,
                                                        const void* __restrict__ fx,
                                                        const unsigned short* __restrict__ alpha,
                                                        void* __restrict__ out) {
    const unsigned off = (blockIdx.x * RZ_BLOCK + threadIdx.x) * 16u;
    const u32x4 fv = __builtin_nontemporal_load((const u32x4*)((const char*)fx + off));
    const u32x4 xv = __builtin_nontemporal_load((const u32x4*)((const char*)x + off));
    const float a = __uint_as_float((unsigned)alpha[0] << 16);
    u32x4 o;
#pragma unroll
    for (int j = 0; j < 4; ++j) o[j] = rz_pair(xv[j], fv[j], a);
    *(u32x4*)((char*)out + off) = o;
}

__global__ __launch_bounds__(256) void rezero_generic(const unsigned short* __restrict__ x,
                                                      const unsigned short* __restrict__ fx,
                                                      const unsigned short* __restrict__ alpha,
                                                      unsigned short* __restrict__ out,
                                                      long long start, long long n) {
    const long long i = start + (long long)blockIdx.x * 256 + threadIdx.x;
    if (i < n) {
        const float a = __uint_as_float((unsigned)alpha[0] << 16);
        out[i] = (unsigned short)(rz_pair(x[i], fx[i], a) & 0xffffu);
    }
}

extern "C" void kernel_launch(void* const* d_in, const int* in_sizes, int n_in, void* d_out,
                              int out_size, void* d_ws, size_t ws_size, hipStream_t stream) {
    (void)in_sizes; (void)n_in; (void)d_ws; (void)ws_size;
    const long long n = out_size;
    const uintptr_t al = (uintptr_t)d_in[0] | (uintptr_t)d_in[1] | (uintptr_t)d_out;
    long long tiles = n / RZ_TILE_ELEMS;
    if (al & 15) tiles = 0;
    if (tiles > 0)
        rezero_main<<<dim3((unsigned)tiles), dim3(RZ_BLOCK), 0, stream>>>(
            d_in[0], d_in[1], (const unsigned short*)d_in[2], d_out);
    const long long done = tiles * RZ_TILE_ELEMS;
    if (done < n)
        rezero_generic<<<dim3((unsigned)((n - done + 255) / 256)), dim3(256), 0, stream>>>(
            (const unsigned short*)d_in[0], (const unsigned short*)d_in[1],
            (const unsigned short*)d_in[2], (unsigned short*)d_out, done, n);
}
```
